```python
import torch

def _softmax(x, axis=-1):
    return torch.softmax(x, dim=axis)
def _gelu(x):
    return torch.nn.functional.gelu(x, approximate="tanh")
def _silu(x):
    return torch.nn.functional.silu(x)
def _sigmoid(x):
    return torch.sigmoid(x)
def _layernorm(x, axis=-1, eps=1e-5):
    m = x.mean(dim=axis, keepdim=True)
    v = x.var(dim=axis, keepdim=True, unbiased=False)
    return (x - m) / torch.sqrt(v + eps)
def _scatter_add(tgt, idx, src, axis=0):
    return tgt.clone().index_add_(axis, idx, src)
def _segment_sum(src, seg, n):
    out = torch.zeros((n,) + src.shape[1:], dtype=src.dtype)
    return out.index_add_(0, seg, src)
def _pad(x, pw):
    flat = tuple(v for pair in reversed(pw) for v in pair)
    return torch.nn.functional.pad(x, flat)

kernel_name = 'gated_mix__softmax_full_mha__dense_activation_squared_relu__prompt'

def setup_inputs():

    _w = (lambda a: (lambda t: t.to(torch.bfloat16) if t.is_floating_point() else t)(torch.as_tensor(a)))
    def _A_setup():
        B, S, D = 1, 2048, 512
        return {
            "x": _w((lambda *s: torch.randn(*s, dtype=torch.bfloat16) * 0.05)(B, S, D)),
            "wqkv": _w((lambda *s: torch.randn(*s, dtype=torch.bfloat16) * 0.8)(D, 3 * D)),
            "wo": _w((lambda *s: torch.randn(*s, dtype=torch.bfloat16) * 0.1)(D, D)),
        }
    _A_in = _A_setup()

    def _B_setup():
        B, S, D, F = 1, 2048, 512, 1792
        return {
            "x": _w((lambda *s: torch.randn(*s, dtype=torch.bfloat16) * 0.05)(B, S, D)),
            "w1": _w((lambda *s: torch.randn(*s, dtype=torch.bfloat16) * 0.1)(D, F)),
            "b1": _w((lambda *s: torch.randn(*s, dtype=torch.bfloat16) * 0.05)(F)),
            "w2": _w((lambda *s: torch.randn(*s, dtype=torch.bfloat16) * 0.05)(F, D)),
            "b2": _w((lambda *s: torch.randn(*s, dtype=torch.bfloat16) * 0.05)(D)),
        }
    _B_in = _B_setup()

    return {
        "A_x": _A_in["x"],
        "A_wqkv": _A_in["wqkv"],
        "A_wo": _A_in["wo"],
        "B_x": _B_in["x"],
        "B_w1": _B_in["w1"],
        "B_b1": _B_in["b1"],
        "B_w2": _B_in["w2"],
        "B_b2": _B_in["b2"],
        "gate_w": _w((lambda *s: torch.randn(*s, dtype=torch.bfloat16) * 0.05)(512) * 16.0),
    }

def reference(A_x, A_wqkv, A_wo, B_x, B_w1, B_b1, B_w2, B_b2, gate_w):
    def _A_ref(x, wqkv, wo):
        B, S, D = x.shape
        H = 32; Dh = D // H
        qkv = (lambda x, *d: x.permute(*d))((x @ wqkv).reshape(B, S, 3, H, Dh), 2, 0, 3, 1, 4)
        q, k, v = qkv[0], qkv[1], qkv[2]
        scale = Dh ** -0.5
        scores = torch.einsum("bhqd,bhkd->bhqk", q, k) * scale
        p = _softmax(scores, axis=-1)
        o = torch.einsum("bhqk,bhkd->bhqd", p, v)
        return (lambda x, *d: x.permute(*d))(o, 0, 2, 1, 3).reshape(B, S, D) @ wo

    def _B_ref(x, w1, b1, w2, b2):
        h = torch.einsum("bsd,df->bsf", x, w1) + b1
        zero = torch.zeros_like(h)
        r = torch.where(h > zero, h, zero)
        h = r * r
        out = torch.einsum("bsf,fd->bsd", h, w2) + b2
        return out

    x = A_x
    g = _sigmoid((x * gate_w).sum(-1, keepdims=True))
    a = _A_ref(x, A_wqkv, A_wo)
    b = _B_ref(x, B_w1, B_b1, B_w2, B_b2)
    return g * a + (1 - g) * b
```

```cpp
#include <hip/hip_runtime.h>
#include <hip/hip_bf16.h>
#include <stdint.h>

typedef __attribute__((ext_vector_type(8))) __bf16 bf16x8;
typedef __attribute__((ext_vector_type(4))) __bf16 bf16x4;
typedef __attribute__((ext_vector_type(16))) float f32x16;
typedef __attribute__((ext_vector_type(8))) float f32x8;
typedef __attribute__((ext_vector_type(4))) float f32x4;
typedef __attribute__((ext_vector_type(4))) short s16x4;
typedef __attribute__((address_space(3))) s16x4 lds_s16x4;

#define DI __device__ __forceinline__
#define MFMA32(a, b, c) __builtin_amdgcn_mfma_f32_32x32x16_bf16((a), (b), (c), 0, 0, 0)

constexpr int S_ = 2048, D_ = 512, F_ = 1792;
constexpr float QSCALE = 0.25f * 1.4426950408889634f;

constexpr size_t MB = 1024 * 1024;
constexpr size_t WS_Q = 0;
constexpr size_t WS_K = 2 * MB;
constexpr size_t WS_V = 4 * MB;
constexpr size_t WS_H = 6 * MB;
constexpr size_t WS_O = 14 * MB;
constexpr size_t WS_B = 16 * MB;
constexpr size_t WS_G = 20 * MB;
constexpr size_t WS_W2B = 21 * MB;
constexpr size_t WS_WOB = 23 * MB;

DI void store_bf16x4(__bf16* p, float a, float b, float c, float d) {
  f32x4 v = {a, b, c, d};
  *reinterpret_cast<bf16x4*>(p) = __builtin_convertvector(v, bf16x4);
}

template <int EPI>
DI void gemm1_tile(const __bf16* __restrict__ x, const __bf16* __restrict__ W, const int ldw, const int m0, const int n0,
                   char* lds, __bf16* __restrict__ dst, const __bf16* __restrict__ bias) {
  const int tid = threadIdx.x, lane = tid & 63, w = tid >> 6, wr = w >> 1, wc = w & 1;
  const int r32 = lane & 31, hh = lane >> 5;
  char* Abuf = lds;
  char* Bbuf = lds + 32768;

  const __bf16* asrc[4];
  const __bf16* bsrc[4];
#pragma unroll
  for (int jj = 0; jj < 4; ++jj) {
    const int j = 4 * w + jj;
    const int row = 8 * j + (lane >> 3);
    const int c = (lane & 7) ^ ((row >> 1) & 7);
    asrc[jj] = x + (size_t)(m0 + row) * D_ + 8 * c;
    const int krow = 4 * j + (lane >> 4);
    const int s16 = lane & 15;
    const int cg = (s16 >> 2) ^ (krow & 3);
    bsrc[jj] = W + (size_t)krow * ldw + n0 + cg * 32 + (s16 & 3) * 8;
  }
  auto stage = [&](int buf, int kt) {
#pragma unroll
    for (int jj = 0; jj < 4; ++jj) {
      __builtin_amdgcn_global_load_lds((const unsigned*)(asrc[jj] + kt * 64),
                                       (unsigned*)(Abuf + buf * 16384 + (4 * w + jj) * 1024 + lane * 16), 16, 0, 0);
      __builtin_amdgcn_global_load_lds((const unsigned*)(bsrc[jj] + (size_t)kt * 64 * ldw),
                                       (unsigned*)(Bbuf + buf * 16384 + (4 * w + jj) * 1024 + lane * 16), 16, 0, 0);
    }
  };
  int aoff[2][4];
#pragma unroll
  for (int st = 0; st < 2; ++st) {
    const int row = wr * 64 + st * 32 + r32;
#pragma unroll
    for (int ks = 0; ks < 4; ++ks) aoff[st][ks] = row * 128 + (((2 * ks + hh) ^ ((row >> 1) & 7)) << 4);
  }
  int boff[2];
  {
    const int q = (lane & 15) >> 2, p = lane & 3, g1 = (lane >> 4) & 1;
#pragma unroll
    for (int nt = 0; nt < 2; ++nt) {
      const int cg = 2 * wc + nt;
      boff[nt] = (8 * hh + q) * 256 + ((cg ^ q) << 6) + g1 * 32 + p * 8;
    }
  }
  f32x16 acc[2][2];
#pragma unroll
  for (int i = 0; i < 2; ++i)
#pragma unroll
    for (int j = 0; j < 2; ++j)
#pragma unroll
      for (int e = 0; e < 16; ++e) acc[i][j][e] = 0.f;

  constexpr int NT = D_ / 64;
  stage(0, 0);
  for (int t = 0; t < NT; ++t) {
    __syncthreads();
    if (t + 1 < NT) stage((t + 1) & 1, t + 1);
    const char* Ab = Abuf + (t & 1) * 16384;
    const char* Bb = Bbuf + (t & 1) * 16384;
#pragma unroll
    for (int ks = 0; ks < 4; ++ks) {
      bf16x8 xf[2], wf[2];
#pragma unroll
      for (int st = 0; st < 2; ++st) xf[st] = *reinterpret_cast<const bf16x8*>(Ab + aoff[st][ks]);
#pragma unroll
      for (int nt = 0; nt < 2; ++nt) {
        const char* bp = Bb + boff[nt] + ks * 4096;
        const s16x4 lo = __builtin_amdgcn_ds_read_tr16_b64_v4i16((lds_s16x4*)(bp));
        const s16x4 hi = __builtin_amdgcn_ds_read_tr16_b64_v4i16((lds_s16x4*)(bp + 1024));
        wf[nt] = __builtin_bit_cast(bf16x8, __builtin_shufflevector(lo, hi, 0, 1, 2, 3, 4, 5, 6, 7));
      }
#pragma unroll
      for (int st = 0; st < 2; ++st)
#pragma unroll
        for (int nt = 0; nt < 2; ++nt) {
          if constexpr (EPI == 2) acc[st][nt] = MFMA32(xf[st], wf[nt], acc[st][nt]);
          else acc[st][nt] = MFMA32(wf[nt], xf[st], acc[st][nt]);
        }
    }
  }

#pragma unroll
  for (int st = 0; st < 2; ++st)
#pragma unroll
    for (int nt = 0; nt < 2; ++nt) {
      const int stile = (m0 + wr * 64 + st * 32) >> 5;
      if constexpr (EPI == 2) {
        const int n = n0 - 1024 + wc * 64 + nt * 32 + r32;
        const int head = n >> 4, d = n & 15;
        __bf16* base = dst + (size_t)head * 32768 + stile * 512 + d * 8 + 4 * hh;
#pragma unroll
        for (int a = 0; a < 4; ++a)
          store_bf16x4(base + (a >> 1) * 256 + (a & 1) * 128, acc[st][nt][4 * a], acc[st][nt][4 * a + 1],
                       acc[st][nt][4 * a + 2], acc[st][nt][4 * a + 3]);
      } else {
        const int s = m0 + wr * 64 + st * 32 + r32;
#pragma unroll
        for (int a = 0; a < 4; ++a) {
          const int n = n0 + wc * 64 + nt * 32 + 8 * a + 4 * hh;
          const float v0 = acc[st][nt][4 * a], v1 = acc[st][nt][4 * a + 1], v2 = acc[st][nt][4 * a + 2],
                      v3 = acc[st][nt][4 * a + 3];
          if constexpr (EPI == 0) {
            const int head = n >> 4, d0 = n & 15;
            store_bf16x4(dst + ((size_t)head * S_ + s) * 16 + d0, v0 * QSCALE, v1 * QSCALE, v2 * QSCALE, v3 * QSCALE);
          } else if constexpr (EPI == 1) {
            const int nn = n - 512, head = nn >> 4, d0 = nn & 15;
            store_bf16x4(dst + (size_t)head * 32768 + stile * 512 + (d0 >> 3) * 256 + r32 * 8 + (d0 & 7), v0, v1, v2, v3);
          } else {
            const bf16x4 bb = *reinterpret_cast<const bf16x4*>(bias + n);
            float h0 = fmaxf(v0 + (float)bb[0], 0.f), h1 = fmaxf(v1 + (float)bb[1], 0.f);
            float h2 = fmaxf(v2 + (float)bb[2], 0.f), h3 = fmaxf(v3 + (float)bb[3], 0.f);
            store_bf16x4(dst + ((size_t)stile * 224 + (n >> 3)) * 256 + r32 * 8 + (n & 7), h0 * h0, h1 * h1, h2 * h2,
                         h3 * h3);
          }
        }
      }
    }
}

DI void reblock(const __bf16* __restrict__ W, __bf16* __restrict__ out, int nchunks_k, int idx, int total) {
  for (; idx < total; idx += 256 * 32) {
    const int n = idx & 511, kc = idx >> 9;
    bf16x8 v;
#pragma unroll
    for (int j = 0; j < 8; ++j) v[j] = W[(size_t)(8 * kc + j) * 512 + n];
    *reinterpret_cast<bf16x8*>(out + ((size_t)(n >> 5) * nchunks_k + kc) * 256 + (n & 31) * 8) = v;
  }
}

constexpr int K1_GEMM_BLOCKS = 16 * 26;
constexpr int K1_GATE_BLOCKS = 16;
constexpr int K1_RB_BLOCKS = 32;

__global__ __launch_bounds__(256, 2) void k1_proj(const __bf16* __restrict__ x, const __bf16* __restrict__ wqkv,
                                                  const __bf16* __restrict__ w1, const __bf16* __restrict__ b1,
                                                  const __bf16* __restrict__ w2, const __bf16* __restrict__ wo,
                                                  const __bf16* __restrict__ gate_w, char* __restrict__ ws) {
  __shared__ __attribute__((aligned(16))) char lds[65536];
  const int b = blockIdx.x;
  if (b < K1_GEMM_BLOCKS) {
    const int rt = b & 15, ct = b >> 4;
    const int m0 = rt * 128;
    if (ct < 4) gemm1_tile<0>(x, wqkv, 1536, m0, ct * 128, lds, (__bf16*)(ws + WS_Q), nullptr);
    else if (ct < 8) gemm1_tile<1>(x, wqkv, 1536, m0, ct * 128, lds, (__bf16*)(ws + WS_K), nullptr);
    else if (ct < 12) gemm1_tile<2>(x, wqkv, 1536, m0, ct * 128, lds, (__bf16*)(ws + WS_V), nullptr);
    else gemm1_tile<3>(x, w1, F_, m0, (ct - 12) * 128, lds, (__bf16*)(ws + WS_H), b1);
  } else if (b < K1_GEMM_BLOCKS + K1_GATE_BLOCKS) {
    const int lane = threadIdx.x & 63;
    const int gw = (b - K1_GEMM_BLOCKS) * 4 + (threadIdx.x >> 6);
    const bf16x8 gv = *reinterpret_cast<const bf16x8*>(gate_w + lane * 8);
    float* G = (float*)(ws + WS_G);
    for (int row = gw; row < S_; row += K1_GATE_BLOCKS * 4) {
      const bf16x8 xv = *reinterpret_cast<const bf16x8*>(x + (size_t)row * D_ + lane * 8);
      float acc = 0.f;
#pragma unroll
      for (int j = 0; j < 8; ++j) acc = fmaf((float)xv[j], (float)gv[j], acc);
#pragma unroll
      for (int off = 32; off > 0; off >>= 1) acc += __shfl_xor(acc, off);
      if (lane == 0) G[row] = 1.f / (1.f + __expf(-acc));
    }
  } else {
    const int rb = b - K1_GEMM_BLOCKS - K1_GATE_BLOCKS;
    const int idx = rb * 256 + threadIdx.x;
    reblock(w2, (__bf16*)(ws + WS_W2B), 224, idx, 224 * 512);
    reblock(wo, (__bf16*)(ws + WS_WOB), 64, idx, 64 * 512);
  }
}

constexpr int LDS_K = 0, LDS_V = 65536, LDS_RED = 131072, LDS_FLAG = 131072 + 16384, LDS_TOTAL = LDS_FLAG + 64;

DI void lds_signal(unsigned* flag, int lane) {
  if (lane == 0) __hip_atomic_fetch_add(flag, 1u, __ATOMIC_RELAXED, __HIP_MEMORY_SCOPE_WORKGROUP);
}
DI void lds_wait_ge(unsigned* flag, unsigned target) {
  for (int spin = 0; spin < (1 << 22); ++spin) {
    if (__hip_atomic_load(flag, __ATOMIC_RELAXED, __HIP_MEMORY_SCOPE_WORKGROUP) >= target) break;
    __builtin_amdgcn_s_sleep(2);
  }
  __builtin_amdgcn_fence(__ATOMIC_ACQUIRE, "workgroup");
}

DI bf16x8 pack8(const f32x16& p, int base) {
  f32x8 v = {p[base], p[base + 1], p[base + 2], p[base + 3], p[base + 4], p[base + 5], p[base + 6], p[base + 7]};
  return __builtin_convertvector(v, bf16x8);
}

DI void attn_store(const f32x16& O, float l, __bf16* __restrict__ Os, int stile, int head, int r32, int hh) {
  const float inv = 1.f / l;
  __bf16* base = Os + ((size_t)stile * 64 + 2 * head) * 256 + r32 * 8 + 4 * hh;
  store_bf16x4(base, O[0] * inv, O[1] * inv, O[2] * inv, O[3] * inv);
  store_bf16x4(base + 256, O[4] * inv, O[5] * inv, O[6] * inv, O[7] * inv);
}

DI void attn_waves(char* lds, const char* __restrict__ ws, int w, int lane, int head, int qb) {
  const int r32 = lane & 31, hh = lane >> 5;
  unsigned* flags = (unsigned*)(lds + LDS_FLAG);
  const int q0 = qb * 256 + w * 32;
  const __bf16* Qs = (const __bf16*)(ws + WS_Q);
  const bf16x8 qf = *reinterpret_cast<const bf16x8*>(Qs + ((size_t)head * S_ + q0 + r32) * 16 + 8 * hh);

  const char* ksrc = ws + WS_K + (size_t)head * 65536 + lane * 16;
  const char* vsrc = ws + WS_V + (size_t)head * 65536 + lane * 16;
#pragma unroll
  for (int s = 0; s < 4; ++s) {
#pragma unroll
    for (int i = 0; i < 2; ++i) {
      const int off = s * 16384 + (w + 8 * i) * 1024;
      __builtin_amdgcn_global_load_lds((const unsigned*)(ksrc + off), (unsigned*)(lds + LDS_K + off + lane * 16), 16, 0, 0);
      __builtin_amdgcn_global_load_lds((const unsigned*)(vsrc + off), (unsigned*)(lds + LDS_V + off + lane * 16), 16, 0, 0);
    }
  }

  const int kperm = (r32 & 0x13) | ((r32 & 4) << 1) | ((r32 & 8) >> 1);
  const char* kbase = lds + LDS_K + hh * 512 + kperm * 16;
  const char* vbase = lds + LDS_V + hh * 256 + (r32 & 15) * 16;
  const bool vact = r32 < 16;
  bf16x8 vconst;
  {
    const __bf16 cv = (r32 == 16 || r32 == 20) ? (__bf16)1.0f : (__bf16)0.0f;
#pragma unroll
    for (int j = 0; j < 8; ++j) vconst[j] = cv;
  }
  f32x16 zero;
#pragma unroll
  for (int i = 0; i < 16; ++i) zero[i] = 0.f;

  asm volatile("s_waitcnt vmcnt(12)" ::: "memory");
  lds_signal(&flags[0], lane);
  lds_wait_ge(&flags[0], 8);

  bf16x8 kf = *reinterpret_cast<const bf16x8*>(kbase);
  f32x16 cinit;
  {
    const f32x16 s0 = MFMA32(kf, qf, zero);
    float m = s0[0];
#pragma unroll
    for (int i = 1; i < 16; ++i) m = fmaxf(m, s0[i]);
    m = fmaxf(m, __shfl_xor(m, 32));
#pragma unroll
    for (int i = 0; i < 16; ++i) cinit[i] = -m;
  }

  f32x16 O = zero;
  bf16x8 vf0 = vconst, vf1 = vconst;
  if (vact) {
    vf0 = *reinterpret_cast<const bf16x8*>(vbase);
    vf1 = *reinterpret_cast<const bf16x8*>(vbase + 512);
  }
  f32x16 SA = MFMA32(kf, qf, cinit), SB;
  kf = *reinterpret_cast<const bf16x8*>(kbase + 1024);
  for (int t = 0; t < 64; t += 2) {
    if ((t & 15) == 14 && t < 48) {
      const int s = (t >> 4) + 1;
      if (s == 1) asm volatile("s_waitcnt vmcnt(8)" ::: "memory");
      else if (s == 2) asm volatile("s_waitcnt vmcnt(4)" ::: "memory");
      else asm volatile("s_waitcnt vmcnt(0)" ::: "memory");
      lds_signal(&flags[s], lane);
      lds_wait_ge(&flags[s], 8);
    }
    SB = MFMA32(kf, qf, cinit);
    kf = *reinterpret_cast<const bf16x8*>(kbase + (t + 2) * 1024);
#pragma unroll
    for (int i = 0; i < 16; ++i) SA[i] = __builtin_amdgcn_exp2f(SA[i]);
    O = MFMA32(vf0, pack8(SA, 0), O);
    O = MFMA32(vf1, pack8(SA, 8), O);
    if (vact) {
      vf0 = *reinterpret_cast<const bf16x8*>(vbase + (t + 1) * 1024);
      vf1 = *reinterpret_cast<const bf16x8*>(vbase + (t + 1) * 1024 + 512);
    }
    SA = MFMA32(kf, qf, cinit);
    kf = *reinterpret_cast<const bf16x8*>(kbase + (t + 3) * 1024);
#pragma unroll
    for (int i = 0; i < 16; ++i) SB[i] = __builtin_amdgcn_exp2f(SB[i]);
    O = MFMA32(vf0, pack8(SB, 0), O);
    O = MFMA32(vf1, pack8(SB, 8), O);
    if (vact) {
      vf0 = *reinterpret_cast<const bf16x8*>(vbase + (t + 2) * 1024);
      vf1 = *reinterpret_cast<const bf16x8*>(vbase + (t + 2) * 1024 + 512);
    }
  }
  float l = O[8];
  bool bad = !(l < 1e30f);
#pragma unroll
  for (int i = 0; i < 8; ++i) bad |= !(fabsf(O[i]) < 3e38f);

  if (__any(bad)) {
    float m = -INFINITY;
    O = zero;
    for (int t = 0; t < 64; ++t) {
      const bf16x8 k8 = *reinterpret_cast<const bf16x8*>(kbase + t * 1024);
      if (vact) {
        vf0 = *reinterpret_cast<const bf16x8*>(vbase + t * 1024);
        vf1 = *reinterpret_cast<const bf16x8*>(vbase + t * 1024 + 512);
      }
      f32x16 s = MFMA32(k8, qf, zero);
      float tm = s[0];
#pragma unroll
      for (int i = 1; i < 16; ++i) tm = fmaxf(tm, s[i]);
      tm = fmaxf(tm, __shfl_xor(tm, 32));
      const float mn = fmaxf(m, tm);
      const float alpha = __builtin_amdgcn_exp2f(m - mn);
      m = mn;
#pragma unroll
      for (int i = 0; i < 9; ++i) O[i] *= alpha;
#pragma unroll
      for (int i = 0; i < 16; ++i) s[i] = __builtin_amdgcn_exp2f(s[i] - mn);
      O = MFMA32(vf0, pack8(s, 0), O);
      O = MFMA32(vf1, pack8(s, 8), O);
    }
    l = O[8];
  }
  attn_store(O, l, (__bf16*)(ws + WS_O), q0 >> 5, head, r32, hh);
}

DI void ffn2_waves(char* lds, char* __restrict__ ws, const __bf16* __restrict__ b2, int g, int lane, int rb, int cb) {
  const int r32 = lane & 31, hh = lane >> 5;
  const __bf16* Hs = (const __bf16*)(ws + WS_H);
  const __bf16* W2 = (const __bf16*)(ws + WS_W2B);
  const __bf16* ap = Hs + ((size_t)(2 * rb) * 224 + 28 * g + hh) * 256 + r32 * 8;
  const __bf16* bp = W2 + ((size_t)(2 * cb) * 224 + 28 * g + hh) * 256 + r32 * 8;
  constexpr size_t TSTRIDE = (size_t)224 * 256;
  f32x16 acc[2][2];
#pragma unroll
  for (int i = 0; i < 2; ++i)
#pragma unroll
    for (int j = 0; j < 2; ++j)
#pragma unroll
      for (int e = 0; e < 16; ++e) acc[i][j][e] = 0.f;
#pragma unroll 2
  for (int ks = 0; ks < 14; ++ks) {
    bf16x8 hf[2], wf[2];
#pragma unroll
    for (int i = 0; i < 2; ++i) {
      hf[i] = *reinterpret_cast<const bf16x8*>(ap + i * TSTRIDE + ks * 512);
      wf[i] = *reinterpret_cast<const bf16x8*>(bp + i * TSTRIDE + ks * 512);
    }
#pragma unroll
    for (int st = 0; st < 2; ++st)
#pragma unroll
      for (int nt = 0; nt < 2; ++nt) acc[st][nt] = MFMA32(wf[nt], hf[st], acc[st][nt]);
  }
  float* red = (float*)(lds + LDS_RED);
  unsigned* turn = (unsigned*)(lds + LDS_FLAG) + 8;
  if (g > 0) {
    lds_wait_ge(turn, (unsigned)g);
#pragma unroll
    for (int i = 0; i < 2; ++i)
#pragma unroll
      for (int j = 0; j < 2; ++j)
#pragma unroll
        for (int e = 0; e < 16; ++e) acc[i][j][e] += red[((i * 2 + j) * 16 + e) * 64 + lane];
  }
  if (g < 7) {
#pragma unroll
    for (int i = 0; i < 2; ++i)
#pragma unroll
      for (int j = 0; j < 2; ++j)
#pragma unroll
        for (int e = 0; e < 16; ++e) red[((i * 2 + j) * 16 + e) * 64 + lane] = acc[i][j][e];
    __builtin_amdgcn_fence(__ATOMIC_RELEASE, "workgroup");
    lds_signal(turn, lane);
  } else {
    float* bbase = (float*)(ws + WS_B) + (((size_t)(2 * rb) * 128 + cb * 16 + hh) * 32 + r32) * 4;
    const __bf16* bias = b2 + cb * 64 + 4 * hh;
#pragma unroll
    for (int st = 0; st < 2; ++st)
#pragma unroll
      for (int nt = 0; nt < 2; ++nt)
#pragma unroll
        for (int a = 0; a < 4; ++a) {
          const bf16x4 bb = *reinterpret_cast<const bf16x4*>(bias + nt * 32 + 8 * a);
          f32x4 v = {acc[st][nt][4 * a] + (float)bb[0], acc[st][nt][4 * a + 1] + (float)bb[1],
                     acc[st][nt][4 * a + 2] + (float)bb[2], acc[st][nt][4 * a + 3] + (float)bb[3]};
          *reinterpret_cast<f32x4*>(bbase + st * 16384 + nt * 1024 + a * 256) = v;
        }
  }
}

__global__ __launch_bounds__(1024) void k2_attn_ffn2(char* __restrict__ ws, const __bf16* __restrict__ b2) {
  __shared__ __attribute__((aligned(16))) char lds[LDS_TOTAL];
  const int lane = threadIdx.x & 63;
  const int w = __builtin_amdgcn_readfirstlane(threadIdx.x >> 6);
  const int b = blockIdx.x;
  if (threadIdx.x < 16) ((unsigned*)(lds + LDS_FLAG))[threadIdx.x] = 0u;
  __syncthreads();
  const int xcd = b & 7, i = b >> 3;
  if (w < 8) {
    attn_waves(lds, ws, w, lane,  4 * xcd + (i >> 3),  i & 7);
  } else {
    ffn2_waves(lds, ws, b2, w - 8, lane,  4 * xcd + (i >> 3),  i & 7);
  }
}

__global__ __launch_bounds__(256) void k3_out(const char* __restrict__ ws, __bf16* __restrict__ out) {
  const int lane = threadIdx.x & 63, w = threadIdx.x >> 6;
  const int r32 = lane & 31, hh = lane >> 5;
  const int b = blockIdx.x;
  const int stile = (b >> 3) * 2 + (w >> 1);
  const int ntile = (b & 7) * 2 + (w & 1);
  const __bf16* Os = (const __bf16*)(ws + WS_O);
  const __bf16* WO = (const __bf16*)(ws + WS_WOB);
  const __bf16* ap = Os + ((size_t)stile * 64 + hh) * 256 + r32 * 8;
  const __bf16* bp = WO + ((size_t)ntile * 64 + hh) * 256 + r32 * 8;
  f32x16 acc;
#pragma unroll
  for (int e = 0; e < 16; ++e) acc[e] = 0.f;
#pragma unroll 8
  for (int ks = 0; ks < 32; ++ks) {
    const bf16x8 of = *reinterpret_cast<const bf16x8*>(ap + ks * 512);
    const bf16x8 wf = *reinterpret_cast<const bf16x8*>(bp + ks * 512);
    acc = MFMA32(wf, of, acc);
  }
  const int s = stile * 32 + r32;
  const float g = ((const float*)(ws + WS_G))[s];
  const float* Bs = (const float*)(ws + WS_B);
#pragma unroll
  for (int a = 0; a < 4; ++a) {
    const int n = ntile * 32 + 8 * a + 4 * hh;
    const f32x4 bv = *reinterpret_cast<const f32x4*>(Bs + (((size_t)stile * 128 + (n >> 2)) * 32 + r32) * 4);
    store_bf16x4(out + (size_t)s * D_ + n, g * acc[4 * a] + (1.f - g) * bv[0], g * acc[4 * a + 1] + (1.f - g) * bv[1],
                 g * acc[4 * a + 2] + (1.f - g) * bv[2], g * acc[4 * a + 3] + (1.f - g) * bv[3]);
  }
}

extern "C" void kernel_launch(void* const* d_in, const int* in_sizes, int n_in, void* d_out, int out_size, void* d_ws,
                              size_t ws_size, hipStream_t stream) {
  const __bf16* x = (const __bf16*)d_in[0];
  const __bf16* wqkv = (const __bf16*)d_in[1];
  const __bf16* wo = (const __bf16*)d_in[2];
  const __bf16* w1 = (const __bf16*)d_in[4];
  const __bf16* b1 = (const __bf16*)d_in[5];
  const __bf16* w2 = (const __bf16*)d_in[6];
  const __bf16* b2 = (const __bf16*)d_in[7];
  const __bf16* gate_w = (const __bf16*)d_in[8];
  char* ws = (char*)d_ws;
  k1_proj<<<K1_GEMM_BLOCKS + K1_GATE_BLOCKS + K1_RB_BLOCKS, 256, 0, stream>>>(x, wqkv, w1, b1, w2, wo, gate_w, ws);
  k2_attn_ffn2<<<256, 1024, 0, stream>>>(ws, b2);
  k3_out<<<256, 256, 0, stream>>>(ws, (__bf16*)d_out);
}
```

```cpp
#include <hip/hip_runtime.h>
#include <hip/hip_bf16.h>
#include <stdint.h>

typedef __attribute__((ext_vector_type(8))) __bf16 bf16x8;
typedef __attribute__((ext_vector_type(4))) __bf16 bf16x4;
typedef __attribute__((ext_vector_type(16))) float f32x16;
typedef __attribute__((ext_vector_type(8))) float f32x8;
typedef __attribute__((ext_vector_type(4))) float f32x4;
typedef __attribute__((ext_vector_type(4))) short s16x4;
typedef __attribute__((address_space(3))) s16x4 lds_s16x4;

#define DI __device__ __forceinline__
#define MFMA32(a, b, c) __builtin_amdgcn_mfma_f32_32x32x16_bf16((a), (b), (c), 0, 0, 0)

constexpr int S_ = 2048, D_ = 512, F_ = 1792;
constexpr float QSCALE = 0.25f * 1.4426950408889634f;

constexpr size_t MB = 1024 * 1024;
constexpr size_t WS_Q = 0;
constexpr size_t WS_K = 2 * MB;
constexpr size_t WS_V = 4 * MB;
constexpr size_t WS_H = 6 * MB;
constexpr size_t WS_O = 14 * MB;
constexpr size_t WS_B = 16 * MB;
constexpr size_t WS_G = 20 * MB;
constexpr size_t WS_W2B = 21 * MB;
constexpr size_t WS_WOB = 23 * MB;

DI void store_bf16x4(__bf16* p, float a, float b, float c, float d) {
  f32x4 v = {a, b, c, d};
  *reinterpret_cast<bf16x4*>(p) = __builtin_convertvector(v, bf16x4);
}

constexpr int K1_STAGE = 49152;

template <int EPI>
DI void gemm1_tile(const __bf16* __restrict__ x, const __bf16* __restrict__ W, const int ldw, const int m0, const int n0,
                   char* lds, __bf16* __restrict__ dst, const __bf16* __restrict__ bias) {
  const int tid = threadIdx.x, lane = tid & 63, w = tid >> 6, wr = w >> 3, wc = w & 7;
  const int r32 = lane & 31, hh = lane >> 5;

  const __bf16* asrc;
  const __bf16* bsrc[2];
  {
    const int row = 8 * w + (lane >> 3);
    const int c = (lane & 7) ^ ((row >> 1) & 7);
    asrc = x + (size_t)(m0 + row) * D_ + 8 * c;
#pragma unroll
    for (int jj = 0; jj < 2; ++jj) {
      const int krow = 2 * (w + 16 * jj) + (lane >> 5);
      const int s32 = lane & 31;
      const int cg = (s32 >> 2) ^ (krow & 3);
      bsrc[jj] = W + (size_t)krow * ldw + n0 + cg * 32 + (s32 & 3) * 8;
    }
  }
  auto stage = [&](int buf, int kt) {
    char* base = lds + buf * K1_STAGE;
    __builtin_amdgcn_global_load_lds((const unsigned*)(asrc + kt * 64), (unsigned*)(base + w * 1024 + lane * 16), 16, 0, 0);
#pragma unroll
    for (int jj = 0; jj < 2; ++jj)
      __builtin_amdgcn_global_load_lds((const unsigned*)(bsrc[jj] + (size_t)kt * 64 * ldw),
                                       (unsigned*)(base + 16384 + (w + 16 * jj) * 1024 + lane * 16), 16, 0, 0);
  };
  int aoff[2][4];
#pragma unroll
  for (int st = 0; st < 2; ++st) {
    const int row = wr * 64 + st * 32 + r32;
#pragma unroll
    for (int ks = 0; ks < 4; ++ks) aoff[st][ks] = row * 128 + (((2 * ks + hh) ^ ((row >> 1) & 7)) << 4);
  }
  const int boff = 16384 + (8 * hh + ((lane & 15) >> 2)) * 512 + ((wc ^ ((lane & 15) >> 2)) << 6) + ((lane >> 4) & 1) * 32 +
                   (lane & 3) * 8;
  f32x16 acc[2];
#pragma unroll
  for (int i = 0; i < 2; ++i)
#pragma unroll
    for (int e = 0; e < 16; ++e) acc[i][e] = 0.f;

  constexpr int NT = D_ / 64;
  stage(0, 0);
  stage(1, 1);
  for (int t = 0; t < NT; ++t) {
    if (t + 1 < NT) asm volatile("s_waitcnt vmcnt(3)" ::: "memory");
    else asm volatile("s_waitcnt vmcnt(0)" ::: "memory");
    __builtin_amdgcn_s_barrier();
    if (t + 2 < NT) stage((t + 2) % 3, t + 2);
    const char* Sb = lds + (t % 3) * K1_STAGE;
#pragma unroll
    for (int ks = 0; ks < 4; ++ks) {
      bf16x8 xf[2];
#pragma unroll
      for (int st = 0; st < 2; ++st) xf[st] = *reinterpret_cast<const bf16x8*>(Sb + aoff[st][ks]);
      const char* bp = Sb + boff + ks * 8192;
      const s16x4 lo = __builtin_amdgcn_ds_read_tr16_b64_v4i16((lds_s16x4*)(bp));
      const s16x4 hi = __builtin_amdgcn_ds_read_tr16_b64_v4i16((lds_s16x4*)(bp + 2048));
      const bf16x8 wf = __builtin_bit_cast(bf16x8, __builtin_shufflevector(lo, hi, 0, 1, 2, 3, 4, 5, 6, 7));
#pragma unroll
      for (int st = 0; st < 2; ++st) {
        if constexpr (EPI == 2) acc[st] = MFMA32(xf[st], wf, acc[st]);
        else acc[st] = MFMA32(wf, xf[st], acc[st]);
      }
    }
  }

#pragma unroll
  for (int st = 0; st < 2; ++st) {
    const int stile = (m0 + wr * 64 + st * 32) >> 5;
    if constexpr (EPI == 2) {
      const int n = n0 - 1024 + wc * 32 + r32;
      const int head = n >> 4, d = n & 15;
      __bf16* base = dst + (size_t)head * 32768 + stile * 512 + d * 8 + 4 * hh;
#pragma unroll
      for (int a = 0; a < 4; ++a)
        store_bf16x4(base + (a >> 1) * 256 + (a & 1) * 128, acc[st][4 * a], acc[st][4 * a + 1], acc[st][4 * a + 2],
                     acc[st][4 * a + 3]);
    } else {
      const int s = m0 + wr * 64 + st * 32 + r32;
#pragma unroll
      for (int a = 0; a < 4; ++a) {
        const int n = n0 + wc * 32 + 8 * a + 4 * hh;
        const float v0 = acc[st][4 * a], v1 = acc[st][4 * a + 1], v2 = acc[st][4 * a + 2], v3 = acc[st][4 * a + 3];
        if constexpr (EPI == 0) {
          const int head = n >> 4, d0 = n & 15;
          store_bf16x4(dst + ((size_t)head * S_ + s) * 16 + d0, v0 * QSCALE, v1 * QSCALE, v2 * QSCALE, v3 * QSCALE);
        } else if constexpr (EPI == 1) {
          const int nn = n - 512, head = nn >> 4, d0 = nn & 15;
          store_bf16x4(dst + (size_t)head * 32768 + stile * 512 + (d0 >> 3) * 256 + r32 * 8 + (d0 & 7), v0, v1, v2, v3);
        } else {
          const bf16x4 bb = *reinterpret_cast<const bf16x4*>(bias + n);
          float h0 = fmaxf(v0 + (float)bb[0], 0.f), h1 = fmaxf(v1 + (float)bb[1], 0.f);
          float h2 = fmaxf(v2 + (float)bb[2], 0.f), h3 = fmaxf(v3 + (float)bb[3], 0.f);
          store_bf16x4(dst + ((size_t)stile * 224 + (n >> 3)) * 256 + r32 * 8 + (n & 7), h0 * h0, h1 * h1, h2 * h2,
                       h3 * h3);
        }
      }
    }
  }
}

DI void reblock8(const __bf16* __restrict__ W, __bf16* __restrict__ out, int nck, int kc, int n8) {
  bf16x8 r[8];
#pragma unroll
  for (int j = 0; j < 8; ++j) r[j] = *reinterpret_cast<const bf16x8*>(W + (size_t)(8 * kc + j) * 512 + 8 * n8);
#pragma unroll
  for (int e = 0; e < 8; ++e) {
    bf16x8 v;
#pragma unroll
    for (int j = 0; j < 8; ++j) v[j] = r[j][e];
    const int n = 8 * n8 + e;
    *reinterpret_cast<bf16x8*>(out + ((size_t)(n >> 5) * nck + kc) * 256 + (n & 31) * 8) = v;
  }
}

constexpr int K1_GEMM_BLOCKS = 16 * 13;
constexpr int K1_GATE_BLOCKS = 8;
constexpr int K1_RB_BLOCKS = 18;

__global__ __launch_bounds__(1024) void k1_proj(const __bf16* __restrict__ x, const __bf16* __restrict__ wqkv,
                                                const __bf16* __restrict__ w1, const __bf16* __restrict__ b1,
                                                const __bf16* __restrict__ w2, const __bf16* __restrict__ wo,
                                                const __bf16* __restrict__ gate_w, char* __restrict__ ws) {
  __shared__ __attribute__((aligned(16))) char lds[3 * K1_STAGE];
  const int b = blockIdx.x;
  if (b < K1_GEMM_BLOCKS) {
    const int L = (b & 7) * 26 + (b >> 3);
    const int rt = L & 15, ct = L >> 4;
    const int m0 = rt * 128;
    if (ct < 2) gemm1_tile<0>(x, wqkv, 1536, m0, ct * 256, lds, (__bf16*)(ws + WS_Q), nullptr);
    else if (ct < 4) gemm1_tile<1>(x, wqkv, 1536, m0, ct * 256, lds, (__bf16*)(ws + WS_K), nullptr);
    else if (ct < 6) gemm1_tile<2>(x, wqkv, 1536, m0, ct * 256, lds, (__bf16*)(ws + WS_V), nullptr);
    else gemm1_tile<3>(x, w1, F_, m0, (ct - 6) * 256, lds, (__bf16*)(ws + WS_H), b1);
  } else if (b < K1_GEMM_BLOCKS + K1_GATE_BLOCKS) {
    const int lane = threadIdx.x & 63;
    const int gw = (b - K1_GEMM_BLOCKS) * 16 + (threadIdx.x >> 6);
    const bf16x8 gv = *reinterpret_cast<const bf16x8*>(gate_w + lane * 8);
    float* G = (float*)(ws + WS_G);
#pragma unroll 4
    for (int row = gw; row < S_; row += K1_GATE_BLOCKS * 16) {
      const bf16x8 xv = *reinterpret_cast<const bf16x8*>(x + (size_t)row * D_ + lane * 8);
      float acc = 0.f;
#pragma unroll
      for (int j = 0; j < 8; ++j) acc = fmaf((float)xv[j], (float)gv[j], acc);
#pragma unroll
      for (int off = 32; off > 0; off >>= 1) acc += __shfl_xor(acc, off);
      if (lane == 0) G[row] = 1.f / (1.f + __expf(-acc));
    }
  } else {
    const int idx = (b - K1_GEMM_BLOCKS - K1_GATE_BLOCKS) * 1024 + threadIdx.x;
    const int kc = idx >> 6, n8 = idx & 63;
    if (kc < 224) reblock8(w2, (__bf16*)(ws + WS_W2B), 224, kc, n8);
    else reblock8(wo, (__bf16*)(ws + WS_WOB), 64, kc - 224, n8);
  }
}

constexpr int LDS_K = 0, LDS_V = 65536, LDS_RED = 131072, LDS_CONST = 131072 + 16384, LDS_FLAG = LDS_CONST + 1024,
              LDS_TOTAL = LDS_FLAG + 64;

DI void lds_signal(unsigned* flag, int lane) {
  if (lane == 0) __hip_atomic_fetch_add(flag, 1u, __ATOMIC_RELAXED, __HIP_MEMORY_SCOPE_WORKGROUP);
}
DI void lds_wait_ge(unsigned* flag, unsigned target) {
  for (int spin = 0; spin < (1 << 22); ++spin) {
    if (__hip_atomic_load(flag, __ATOMIC_RELAXED, __HIP_MEMORY_SCOPE_WORKGROUP) >= target) break;
    __builtin_amdgcn_s_sleep(2);
  }
  __builtin_amdgcn_fence(__ATOMIC_ACQUIRE, "workgroup");
}

DI bf16x8 pack8(const f32x16& p, int base) {
  f32x8 v = {p[base], p[base + 1], p[base + 2], p[base + 3], p[base + 4], p[base + 5], p[base + 6], p[base + 7]};
  return __builtin_convertvector(v, bf16x8);
}
DI bf16x8 lds_ld16(const char* lds, int off) { return *reinterpret_cast<const bf16x8*>(lds + off); }

DI void attn_store(const f32x16& O, float l, __bf16* __restrict__ Os, int stile, int head, int r32, int hh) {
  const float inv = 1.f / l;
  __bf16* base = Os + ((size_t)stile * 64 + 2 * head) * 256 + r32 * 8 + 4 * hh;
  store_bf16x4(base, O[0] * inv, O[1] * inv, O[2] * inv, O[3] * inv);
  store_bf16x4(base + 256, O[4] * inv, O[5] * inv, O[6] * inv, O[7] * inv);
}

#define ATTN_TILE(SCUR, SNXT, KNXT, KNEW, KOFF)                                  \
  do {                                                                           \
    SNXT = MFMA32(KNXT, qf, cinit);                                              \
    asm volatile("" : "+v"(SNXT));    \
    KNEW = lds_ld16(lds, kaddr + (KOFF));                                        \
    __builtin_amdgcn_sched_barrier(0);                                           \
    _Pragma("unroll") for (int i = 0; i < 8; ++i) SCUR[i] = __builtin_amdgcn_exp2f(SCUR[i]); \
    const bf16x8 p0 = pack8(SCUR, 0);                                            \
    O = MFMA32(vf0, p0, O);                                                      \
    _Pragma("unroll") for (int i = 8; i < 16; ++i) SCUR[i] = __builtin_amdgcn_exp2f(SCUR[i]); \
    const bf16x8 p1 = pack8(SCUR, 8);                                            \
    O = MFMA32(vf1, p1, O);                                                      \
    vaddr += vstride;                                                            \
    vf0 = lds_ld16(lds, vaddr);                                                  \
    vf1 = lds_ld16(lds, vaddr + 512);                                            \
    __builtin_amdgcn_sched_barrier(0);                                           \
  } while (0)

DI void attn_waves(char* lds, const char* __restrict__ ws, int w, int lane, int head, int qb) {
  const int r32 = lane & 31, hh = lane >> 5;
  unsigned* flags = (unsigned*)(lds + LDS_FLAG);
  const int q0 = qb * 256 + w * 32;
  const __bf16* Qs = (const __bf16*)(ws + WS_Q);
  const bf16x8 qf = *reinterpret_cast<const bf16x8*>(Qs + ((size_t)head * S_ + q0 + r32) * 16 + 8 * hh);

  const char* ksrc = ws + WS_K + (size_t)head * 65536 + lane * 16;
  const char* vsrc = ws + WS_V + (size_t)head * 65536 + lane * 16;
#pragma unroll
  for (int s = 0; s < 4; ++s) {
#pragma unroll
    for (int i = 0; i < 2; ++i) {
      const int off = s * 16384 + (w + 8 * i) * 1024;
      __builtin_amdgcn_global_load_lds((const unsigned*)(ksrc + off), (unsigned*)(lds + LDS_K + off + lane * 16), 16, 0, 0);
      __builtin_amdgcn_global_load_lds((const unsigned*)(vsrc + off), (unsigned*)(lds + LDS_V + off + lane * 16), 16, 0, 0);
    }
  }

  const int kperm = (r32 & 0x13) | ((r32 & 4) << 1) | ((r32 & 8) >> 1);
  const int kaddr0 = LDS_K + hh * 512 + kperm * 16;
  const bool vact = r32 < 16;
  const int vaddr0 = (vact ? LDS_V : LDS_CONST) + hh * 256 + (r32 & 15) * 16;
  const int vstride = vact ? 1024 : 0;
  f32x16 zero;
#pragma unroll
  for (int i = 0; i < 16; ++i) zero[i] = 0.f;

  asm volatile("s_waitcnt vmcnt(12)" ::: "memory");
  lds_signal(&flags[0], lane);
  lds_wait_ge(&flags[0], 8);

  bf16x8 kfA = lds_ld16(lds, kaddr0), kfB;
  f32x16 cinit;
  {
    const f32x16 s0 = MFMA32(kfA, qf, zero);
    float m = s0[0];
#pragma unroll
    for (int i = 1; i < 16; ++i) m = fmaxf(m, s0[i]);
    m = fmaxf(m, __shfl_xor(m, 32));
#pragma unroll
    for (int i = 0; i < 16; ++i) cinit[i] = -m;
  }

  f32x16 O = zero;
  int vaddr = vaddr0, kaddr = kaddr0;
  bf16x8 vf0 = lds_ld16(lds, vaddr), vf1 = lds_ld16(lds, vaddr + 512);
  f32x16 SA = MFMA32(kfA, qf, cinit), SB;
  kfB = lds_ld16(lds, kaddr + 1024);
  for (int t = 0; t < 64; t += 2) {
    if ((t & 15) == 8 && t < 48) {
      const int s = (t >> 4) + 1;
      if (s == 1) asm volatile("s_waitcnt vmcnt(8)" ::: "memory");
      else if (s == 2) asm volatile("s_waitcnt vmcnt(4)" ::: "memory");
      else asm volatile("s_waitcnt vmcnt(0)" ::: "memory");
      lds_signal(&flags[s], lane);
    }
    if ((t & 15) == 14 && t < 48) lds_wait_ge(&flags[(t >> 4) + 1], 8);
    ATTN_TILE(SA, SB, kfB, kfA, 2048);
    ATTN_TILE(SB, SA, kfA, kfB, 3072);
    kaddr += 2048;
  }
  float l = O[8];
  bool bad = !(l < 1e30f);
#pragma unroll
  for (int i = 0; i < 8; ++i) bad |= !(fabsf(O[i]) < 3e38f);

  if (__any(bad)) {
    float m = -INFINITY;
    O = zero;
    for (int t = 0; t < 64; ++t) {
      const bf16x8 k8 = lds_ld16(lds, kaddr0 + t * 1024);
      const bf16x8 w0 = lds_ld16(lds, vaddr0 + t * vstride), w1 = lds_ld16(lds, vaddr0 + t * vstride + 512);
      f32x16 s = MFMA32(k8, qf, zero);
      float tm = s[0];
#pragma unroll
      for (int i = 1; i < 16; ++i) tm = fmaxf(tm, s[i]);
      tm = fmaxf(tm, __shfl_xor(tm, 32));
      const float mn = fmaxf(m, tm);
      const float alpha = __builtin_amdgcn_exp2f(m - mn);
      m = mn;
#pragma unroll
      for (int i = 0; i < 9; ++i) O[i] *= alpha;
#pragma unroll
      for (int i = 0; i < 16; ++i) s[i] = __builtin_amdgcn_exp2f(s[i] - mn);
      O = MFMA32(w0, pack8(s, 0), O);
      O = MFMA32(w1, pack8(s, 8), O);
    }
    l = O[8];
  }
  attn_store(O, l, (__bf16*)(ws + WS_O), q0 >> 5, head, r32, hh);
}

DI void ffn2_waves(char* lds, char* __restrict__ ws, const __bf16* __restrict__ b2, int g, int lane, int rb, int cb) {
  const int r32 = lane & 31, hh = lane >> 5;
  const __bf16* Hs = (const __bf16*)(ws + WS_H);
  const __bf16* W2 = (const __bf16*)(ws + WS_W2B);
  const __bf16* ap = Hs + ((size_t)(2 * rb) * 224 + 28 * g + hh) * 256 + r32 * 8;
  const __bf16* bp = W2 + ((size_t)(2 * cb) * 224 + 28 * g + hh) * 256 + r32 * 8;
  constexpr size_t TSTRIDE = (size_t)224 * 256;
  f32x16 acc[2][2];
#pragma unroll
  for (int i = 0; i < 2; ++i)
#pragma unroll
    for (int j = 0; j < 2; ++j)
#pragma unroll
      for (int e = 0; e < 16; ++e) acc[i][j][e] = 0.f;
  bf16x8 hf[3][2], wf[3][2];
#pragma unroll
  for (int p = 0; p < 3; ++p)
#pragma unroll
    for (int i = 0; i < 2; ++i) {
      hf[p][i] = *reinterpret_cast<const bf16x8*>(ap + i * TSTRIDE + p * 512);
      wf[p][i] = *reinterpret_cast<const bf16x8*>(bp + i * TSTRIDE + p * 512);
    }
#pragma unroll
  for (int ks = 0; ks < 14; ++ks) {
    const int p = ks % 3;
#pragma unroll
    for (int st = 0; st < 2; ++st)
#pragma unroll
      for (int nt = 0; nt < 2; ++nt) acc[st][nt] = MFMA32(wf[p][nt], hf[p][st], acc[st][nt]);
    if (ks + 3 < 14) {
#pragma unroll
      for (int i = 0; i < 2; ++i) {
        hf[p][i] = *reinterpret_cast<const bf16x8*>(ap + i * TSTRIDE + (ks + 3) * 512);
        wf[p][i] = *reinterpret_cast<const bf16x8*>(bp + i * TSTRIDE + (ks + 3) * 512);
      }
    }
  }
  f32x4* red = (f32x4*)(lds + LDS_RED);
  unsigned* turn = (unsigned*)(lds + LDS_FLAG) + 8;
  if (g > 0) {
    lds_wait_ge(turn, (unsigned)g);
#pragma unroll
    for (int i = 0; i < 2; ++i)
#pragma unroll
      for (int j = 0; j < 2; ++j)
#pragma unroll
        for (int a = 0; a < 4; ++a) {
          const f32x4 v = red[((i * 2 + j) * 4 + a) * 64 + lane];
#pragma unroll
          for (int e = 0; e < 4; ++e) acc[i][j][4 * a + e] += v[e];
        }
  }
  if (g < 7) {
#pragma unroll
    for (int i = 0; i < 2; ++i)
#pragma unroll
      for (int j = 0; j < 2; ++j)
#pragma unroll
        for (int a = 0; a < 4; ++a) {
          const f32x4 v = {acc[i][j][4 * a], acc[i][j][4 * a + 1], acc[i][j][4 * a + 2], acc[i][j][4 * a + 3]};
          red[((i * 2 + j) * 4 + a) * 64 + lane] = v;
        }
    __builtin_amdgcn_fence(__ATOMIC_RELEASE, "workgroup");
    lds_signal(turn, lane);
  } else {
    float* bbase = (float*)(ws + WS_B) + (((size_t)(2 * rb) * 128 + cb * 16 + hh) * 32 + r32) * 4;
    const __bf16* bias = b2 + cb * 64 + 4 * hh;
#pragma unroll
    for (int st = 0; st < 2; ++st)
#pragma unroll
      for (int nt = 0; nt < 2; ++nt)
#pragma unroll
        for (int a = 0; a < 4; ++a) {
          const bf16x4 bb = *reinterpret_cast<const bf16x4*>(bias + nt * 32 + 8 * a);
          f32x4 v = {acc[st][nt][4 * a] + (float)bb[0], acc[st][nt][4 * a + 1] + (float)bb[1],
                     acc[st][nt][4 * a + 2] + (float)bb[2], acc[st][nt][4 * a + 3] + (float)bb[3]};
          *reinterpret_cast<f32x4*>(bbase + st * 16384 + nt * 1024 + a * 256) = v;
        }
  }
}

__global__ __launch_bounds__(1024) void k2_attn_ffn2(char* __restrict__ ws, const __bf16* __restrict__ b2) {
  __shared__ __attribute__((aligned(16))) char lds[LDS_TOTAL];
  const int lane = threadIdx.x & 63;
  const int w = __builtin_amdgcn_readfirstlane(threadIdx.x >> 6);
  const int b = blockIdx.x;
  if (threadIdx.x < 16) ((unsigned*)(lds + LDS_FLAG))[threadIdx.x] = 0u;
  if (threadIdx.x < 64) {
    const unsigned v = ((threadIdx.x & 15) == 0 || (threadIdx.x & 15) == 4) ? 0x3F803F80u : 0u;
    const uint4 q = make_uint4(v, v, v, v);
    *reinterpret_cast<uint4*>(lds + LDS_CONST + threadIdx.x * 16) = q;
  }
  __syncthreads();
  const int xcd = b & 7, i = b >> 3;
  if (w < 8) {
    attn_waves(lds, ws, w, lane,  4 * xcd + (i >> 3),  i & 7);
  } else {
    __builtin_amdgcn_s_setprio(2);
    ffn2_waves(lds, ws, b2, w - 8, lane,  4 * xcd + (i >> 3),  i & 7);
  }
}

__global__ __launch_bounds__(1024) void k3_out(const char* __restrict__ ws, __bf16* __restrict__ out) {
  __shared__ __attribute__((aligned(16))) char lds[131072];
  const int lane = threadIdx.x & 63, w = threadIdx.x >> 6;
  const int r32 = lane & 31, hh = lane >> 5;
  const int b = blockIdx.x;
  const int rbk = b >> 3, cbk = b & 7;
  const char* asrc = ws + WS_O + (size_t)rbk * 65536 + lane * 16;
  const char* bsrc = ws + WS_WOB + (size_t)cbk * 65536 + lane * 16;
#pragma unroll
  for (int i = 0; i < 4; ++i) {
    const int off = (w + 16 * i) * 1024;
    __builtin_amdgcn_global_load_lds((const unsigned*)(asrc + off), (unsigned*)(lds + off + lane * 16), 16, 0, 0);
    __builtin_amdgcn_global_load_lds((const unsigned*)(bsrc + off), (unsigned*)(lds + 65536 + off + lane * 16), 16, 0, 0);
  }
  const int sub = w & 3, kq = w >> 2;
  const int st = sub >> 1, nt = sub & 1;
  const int s = (rbk * 2 + st) * 32 + r32;
  const float g = ((const float*)(ws + WS_G))[s];
  __syncthreads();
  f32x16 acc;
#pragma unroll
  for (int e = 0; e < 16; ++e) acc[e] = 0.f;
  const int aoff = st * 32768 + (16 * kq + hh) * 512 + r32 * 16;
  const int boff = 65536 + nt * 32768 + (16 * kq + hh) * 512 + r32 * 16;
#pragma unroll
  for (int ks = 0; ks < 8; ++ks) {
    const bf16x8 of = *reinterpret_cast<const bf16x8*>(lds + aoff + ks * 1024);
    const bf16x8 wf = *reinterpret_cast<const bf16x8*>(lds + boff + ks * 1024);
    acc = MFMA32(wf, of, acc);
  }
  __syncthreads();
  f32x4* red = (f32x4*)lds;
  if (kq > 0) {
#pragma unroll
    for (int a = 0; a < 4; ++a) {
      const f32x4 v = {acc[4 * a], acc[4 * a + 1], acc[4 * a + 2], acc[4 * a + 3]};
      red[(((kq - 1) * 4 + sub) * 4 + a) * 64 + lane] = v;
    }
  }
  __syncthreads();
  if (kq == 0) {
#pragma unroll
    for (int q = 0; q < 3; ++q)
#pragma unroll
      for (int a = 0; a < 4; ++a) {
        const f32x4 v = red[((q * 4 + sub) * 4 + a) * 64 + lane];
#pragma unroll
        for (int e = 0; e < 4; ++e) acc[4 * a + e] += v[e];
      }
    const int stile = rbk * 2 + st, ntile = cbk * 2 + nt;
    const float* Bs = (const float*)(ws + WS_B);
#pragma unroll
    for (int a = 0; a < 4; ++a) {
      const int n = ntile * 32 + 8 * a + 4 * hh;
      const f32x4 bv = *reinterpret_cast<const f32x4*>(Bs + (((size_t)stile * 128 + (n >> 2)) * 32 + r32) * 4);
      store_bf16x4(out + (size_t)s * D_ + n, g * acc[4 * a] + (1.f - g) * bv[0], g * acc[4 * a + 1] + (1.f - g) * bv[1],
                   g * acc[4 * a + 2] + (1.f - g) * bv[2], g * acc[4 * a + 3] + (1.f - g) * bv[3]);
    }
  }
}

extern "C" void kernel_launch(void* const* d_in, const int* in_sizes, int n_in, void* d_out, int out_size, void* d_ws,
                              size_t ws_size, hipStream_t stream) {
  const __bf16* x = (const __bf16*)d_in[0];
  const __bf16* wqkv = (const __bf16*)d_in[1];
  const __bf16* wo = (const __bf16*)d_in[2];
  const __bf16* w1 = (const __bf16*)d_in[4];
  const __bf16* b1 = (const __bf16*)d_in[5];
  const __bf16* w2 = (const __bf16*)d_in[6];
  const __bf16* b2 = (const __bf16*)d_in[7];
  const __bf16* gate_w = (const __bf16*)d_in[8];
  char* ws = (char*)d_ws;
  k1_proj<<<K1_GEMM_BLOCKS + K1_GATE_BLOCKS + K1_RB_BLOCKS, 1024, 0, stream>>>(x, wqkv, w1, b1, w2, wo, gate_w, ws);
  k2_attn_ffn2<<<256, 1024, 0, stream>>>(ws, b2);
  k3_out<<<256, 1024, 0, stream>>>(ws, (__bf16*)d_out);
}
```
